```python
import torch

def _softmax(x, axis=-1):
    return torch.softmax(x, dim=axis)

def _gelu(x):
    return torch.nn.functional.gelu(x, approximate='tanh')

def _silu(x):
    return torch.nn.functional.silu(x)

def _sigmoid(x):
    return torch.sigmoid(x)

def _layernorm(x, axis=-1, eps=1e-05):
    m = x.mean(dim=axis, keepdim=True)
    v = x.var(dim=axis, keepdim=True, unbiased=False)
    return (x - m) / torch.sqrt(v + eps)

def _scatter_add(tgt, idx, src, axis=0):
    return tgt.clone().index_add_(axis, idx, src)

def _segment_sum(src, seg, n):
    out = torch.zeros((n,) + src.shape[1:], dtype=src.dtype)
    return out.index_add_(0, seg, src)

def _pad(x, pw):
    flat = tuple((v for pair in reversed(pw) for v in pair))
    return torch.nn.functional.pad(x, flat)
kernel_name = 'concat_heads__spectral_gfnet__memory_aug_recurrent_mem_tr__prompt'

def setup_inputs():

    _w = lambda a: (lambda t: t.to(torch.bfloat16) if t.is_floating_point() else t)(torch.as_tensor(a))

    def _A_setup():
        B, S, D = (1, 2048, 512)
        Df = D // 2 + 1
        return {'x': _w((lambda *s: torch.randn(*s, dtype=torch.bfloat16) * 0.05)(B, S, D)), 'w_r': _w((lambda *s: torch.randn(*s, dtype=torch.bfloat16) * 3.2)(S, Df) * 0.02), 'w_i': _w((lambda *s: torch.randn(*s, dtype=torch.bfloat16) * 3.2)(S, Df) * 0.02)}
    _A_in = _A_setup()

    def _B_setup():
        B, S, Dm = (1, 2048, 512)
        H, Dh = (32, 128)
        M, seg = (8, 32)
        seg = min(seg, S)
        S = S // seg * seg
        return {'x': _w((lambda *s: torch.randn(*s, dtype=torch.bfloat16) * 0.05)(B, S, Dm)), 'mem0': _w((lambda *s: torch.randn(*s, dtype=torch.bfloat16) * 0.4)(B, M, Dm)), 'Wq': _w((lambda *s: torch.randn(*s, dtype=torch.bfloat16) * 6.4)(Dm, H, Dh) * Dm ** (-0.5)), 'Wk': _w((lambda *s: torch.randn(*s, dtype=torch.bfloat16) * 6.4)(Dm, H, Dh) * Dm ** (-0.5)), 'Wv': _w((lambda *s: torch.randn(*s, dtype=torch.bfloat16) * 0.4)(Dm, H, Dh) * Dm ** (-0.5)), 'Wo': _w((lambda *s: torch.randn(*s, dtype=torch.bfloat16) * 0.4)(H, Dh, Dm) * (H * Dh) ** (-0.5)), 'seg_len': seg}
    _B_in = _B_setup()
    return {'A_x': _A_in['x'], 'A_w_r': _A_in['w_r'], 'A_w_i': _A_in['w_i'], 'B_x': _B_in['x'], 'B_mem0': _B_in['mem0'], 'B_Wq': _B_in['Wq'], 'B_Wk': _B_in['Wk'], 'B_Wv': _B_in['Wv'], 'B_Wo': _B_in['Wo'], 'B_seg_len': _B_in['seg_len']}

def reference(A_x, A_w_r, A_w_i, B_x, B_mem0, B_Wq, B_Wk, B_Wv, B_Wo, B_seg_len):

    def _A_ref(x, w_r, w_i):
        B, S, D = x.shape
        X = (lambda x, axes=(-2, -1): torch.fft.rfft2(x.float(), dim=axes))(x, axes=(-2, -1))
        W = w_r + 1j * w_i
        X = X * W
        y = (lambda x, s, axes=(-2, -1): torch.fft.irfft2(x, s=s, dim=axes).to(torch.bfloat16))(X, (S, D), axes=(-2, -1))
        return y

    def _B_ref(x, mem0, Wq, Wk, Wv, Wo, seg_len):
        B, S, Dm = x.shape
        H, Dh = (Wq.shape[1], Wq.shape[2])
        M = mem0.shape[1]
        scale = Dh ** (-0.5)
        n_seg = S // seg_len
        L = M + seg_len
        idx = torch.arange(L)
        mask = (idx[:, None] < M) | (idx[:, None] >= idx[None, :])
        mem = mem0
        outs = []
        for s in range(n_seg):
            xs = x[:, s * seg_len:(s + 1) * seg_len, :].clone()
            h = torch.concatenate([mem, xs], axis=1)
            q = torch.einsum('btd,dhe->bhte', h, Wq)
            k = torch.einsum('btd,dhe->bhte', h, Wk)
            v = torch.einsum('btd,dhe->bhte', h, Wv)
            scores = torch.einsum('bhqe,bhke->bhqk', q, k) * scale
            scores = torch.where(mask[None, None], scores, -1000000000.0)
            p = _softmax(scores, axis=-1)
            o = torch.einsum('bhqk,bhke->bhqe', p, v)
            o = torch.einsum('bhte,hed->btd', o, Wo)
            mem = o[:, :M, :].clone()
            outs.append(o[:, M:, :])
        return torch.concatenate(outs, axis=1)
    a = _A_ref(A_x, A_w_r, A_w_i)
    b = _B_ref(B_x, B_mem0, B_Wq, B_Wk, B_Wv, B_Wo, B_seg_len)
    return torch.concatenate([a, b], axis=-1)
```

```cpp
#include <hip/hip_runtime.h>
#include <hip/hip_bf16.h>
#include <stdint.h>

#define LAS __attribute__((address_space(3)))
typedef unsigned short u16;
typedef unsigned u32;
typedef __bf16 v2bf __attribute__((ext_vector_type(2)));
typedef __bf16 v4bf __attribute__((ext_vector_type(4)));
typedef __bf16 v8bf __attribute__((ext_vector_type(8)));
typedef __bf16 v16bf __attribute__((ext_vector_type(16)));
typedef float v2f __attribute__((ext_vector_type(2)));
typedef float v4f __attribute__((ext_vector_type(4)));
typedef u32 v2u __attribute__((ext_vector_type(2)));
typedef u32 v4u __attribute__((ext_vector_type(4)));
typedef u32 v8u __attribute__((ext_vector_type(8)));
typedef short v4s __attribute__((ext_vector_type(4)));

constexpr size_t MiB = 1u << 20;
constexpr size_t WS_CTRL_BYTES = 65536;
constexpr size_t WS_KX = 8 * MiB, WS_VX = 24 * MiB, WS_KM = 40 * MiB, WS_VM = 44 * MiB, WS_O = 48 * MiB, WS_QX = 64 * MiB,
                 WS_F1 = 96 * MiB, WS_F2 = 104 * MiB, WS_DBG = 112 * MiB;
#define CW(line) ((line) * 32)
#define C_CLAIM(x) CW(x)
#define C_OVER CW(8)
#define C_ABORT CW(9)
#define C_HINIT CW(10)
#define C_PAR0 CW(11)
#define C_PAR1 CW(12)
#define C_PROGK(h) CW(16 + (h))
#define C_PROGV(h) CW(48 + (h))
#define C_PROGKM(h) CW(80 + (h))
#define C_PROGVM(h) CW(112 + (h))
#define C_PROGO(h, w) CW(144 + 4 * (h) + (w))
#define C_FFT(k) CW(272 + (k))
constexpr u32 SPIN_CAP = 200000u;

#define D1_CLK 700
#define D2_CLK 600
#define DX_CLK 2200

struct Params {
  const u16* ax; const u16* awr; const u16* awi;
  const u16* bx; const u16* mem0; const u16* wq; const u16* wk; const u16* wv; const u16* wo;
  u16* out; char* ws;
};

__device__ __forceinline__ float u2f(u32 x) { return __uint_as_float(x); }
__device__ __forceinline__ u32 f2u(float x) { return __float_as_uint(x); }
__device__ __forceinline__ u32 rfl(u32 x) { return __builtin_amdgcn_readfirstlane(x); }
__device__ __forceinline__ u32 pack2(float a, float b) { v2f t = {a, b}; return __builtin_bit_cast(u32, __builtin_convertvector(t, v2bf)); }
__device__ __forceinline__ u32 ld_sc1(const void* p) { u32 r; asm volatile("global_load_dword %0, %1, off sc1\n\ts_waitcnt vmcnt(0)" : "=&v"(r) : "v"(p) : "memory"); return r; }
__device__ __forceinline__ void st_sc1(void* p, u32 v) { asm volatile("global_store_dword %0, %1, off sc1" :: "v"(p), "v"(v) : "memory"); }
__device__ __forceinline__ void st8_sc1(void* p, v2u v) { asm volatile("global_store_dwordx2 %0, %1, off sc1" :: "v"(p), "v"(v) : "memory"); }
__device__ __forceinline__ void st16_sc1(void* p, v4u v) { asm volatile("global_store_dwordx4 %0, %1, off sc1\n\ts_nop 1" :: "v"(p), "v"(v) : "memory"); }
__device__ __forceinline__ void wait_vm0() { asm volatile("s_waitcnt vmcnt(0)" ::: "memory"); }
__device__ __forceinline__ void wait_lgkm0() { asm volatile("s_waitcnt lgkmcnt(0)" ::: "memory"); }
__device__ __forceinline__ void bar() { asm volatile("s_waitcnt lgkmcnt(0)\n\ts_barrier" ::: "memory"); }
__device__ __forceinline__ u32 clk() { unsigned long long t; asm volatile("s_memtime %0\n\ts_waitcnt lgkmcnt(0)" : "=s"(t) :: "memory"); return (u32)t; }
__device__ __forceinline__ void wait_until(u32 t) { while ((int)(clk() - t) < 0) {} }
__device__ __forceinline__ u32 xcc_id() { return (u32)__builtin_amdgcn_s_getreg(20 | (3 << 11)) & 7u; }
__device__ __forceinline__ bool spin_fail(u32& n, u32* ctrl) {
  ++n;
  if ((n & 255u) != 0u) return false;
  if (rfl(ld_sc1(ctrl + C_ABORT)) != 0u) return true;
  if (n > SPIN_CAP) { st_sc1(ctrl + C_ABORT, 1u); return true; }
  return false;
}
__device__ __forceinline__ u32 wait_ge(u32* ctrl, int word, u32 target) {
  u32 n = 0, v;
  while ((v = rfl(ld_sc1(ctrl + word))) < target) { if (spin_fail(n, ctrl)) break; __builtin_amdgcn_s_sleep(2); }
  return v;
}
__device__ __forceinline__ void glds16(const void* gsrc, u32 lds_dst) {
  u32 keep;
  asm volatile("s_mov_b32 %0, m0\n\ts_mov_b32 m0, %2\n\ts_nop 0\n\tglobal_load_lds_dwordx4 %1, off\n\ts_mov_b32 m0, %0" : "=&s"(keep) : "v"(gsrc), "s"(lds_dst) : "memory");
}
__device__ __forceinline__ v2u tr_read(const char* lds, u32 off) {
  return __builtin_bit_cast(v2u, __builtin_amdgcn_ds_read_tr16_b64_v4i16((LAS v4s*)(lds + off)));
}
__device__ __forceinline__ v4f smf(v8bf a, v16bf b, v4f c, int idx) { return __builtin_amdgcn_smfmac_f32_16x16x64_bf16(a, b, c, idx, 0, 0); }
__device__ __forceinline__ v4f mfma(v8bf a, v8bf b, v4f c) { return __builtin_amdgcn_mfma_f32_16x16x32_bf16(a, b, c, 0, 0, 0); }
__device__ __forceinline__ v4f half_sum(v4f x, v4f y) {
  v4f r;
#pragma unroll
  for (int i = 0; i < 4; ++i) { auto s = __builtin_amdgcn_permlane32_swap(f2u(x[i]), f2u(y[i]), false, false); r[i] = u2f(s[0]) + u2f(s[1]); }
  return r;
}
__device__ __forceinline__ float xmax16(float x) { auto s = __builtin_amdgcn_permlane16_swap(f2u(x), f2u(x), false, false); return fmaxf(u2f(s[0]), u2f(s[1])); }
__device__ __forceinline__ float xmax32(float x) { auto s = __builtin_amdgcn_permlane32_swap(f2u(x), f2u(x), false, false); return fmaxf(u2f(s[0]), u2f(s[1])); }
__device__ __forceinline__ float xsum16(float x) { auto s = __builtin_amdgcn_permlane16_swap(f2u(x), f2u(x), false, false); return u2f(s[0]) + u2f(s[1]); }
__device__ __forceinline__ float xsum32(float x) { auto s = __builtin_amdgcn_permlane32_swap(f2u(x), f2u(x), false, false); return u2f(s[0]) + u2f(s[1]); }

#define DSTAMP(slotidx) do {} while (0)
#define DCOUNT(var) do {} while (0)
#define WSTAMP(k) do {} while (0)
#define DSEG(k) do {} while (0)
constexpr u32 H_MBUF = 0, H_QT = 8192, H_KT = 10240, H_OB = 12288, H_KX = 14336, H_VX = 22528, H_WO = 32768;
constexpr u32 VX_STRIDE = 288;

__device__ __forceinline__ u32 rwoff(u32 rw) { return (rw >> 1) * 16384u + (rw & 1u) * 4096u; }

__device__ __forceinline__ v16bf read_bfrag(const char* lds, u32 base) {
  v2u r1 = tr_read(lds, base), r2 = tr_read(lds, base + 8 * 256), r3 = tr_read(lds, base + 32 * 256), r4 = tr_read(lds, base + 40 * 256);
  v8u t = {r1[0], r2[0], r1[1], r2[1], r3[0], r4[0], r3[1], r4[1]};
  return __builtin_bit_cast(v16bf, t);
}

__device__ __forceinline__ void h_role(const Params& p, char* lds, const int hidx) {
  const int tid = threadIdx.x, l = tid & 63, w = rfl(tid >> 6);
  const int h = hidx >> 2, c = hidx & 3;
  u32* ctrl = (u32*)p.ws;
  char* comm = (char*)(((size_t)p.ws + WS_CTRL_BYTES + (2 * MiB - 1)) & ~(size_t)(2 * MiB - 1));
  const u32 ldsb = (u32)(size_t)(LAS char*)lds;
  DSTAMP(0);

  {
    const v4u ones = {~0u, ~0u, ~0u, ~0u};
    char* mh = comm + (size_t)hidx * 8192 + tid * 16;
    st16_sc1(mh, ones); st16_sc1(mh + 4096, ones);
#pragma unroll
    for (int k = 0; k < 4; ++k) st16_sc1(comm + (1 + (k >> 1)) * MiB + (k & 1) * 8192 + rwoff(hidx) + tid * 16, ones);
  }
  v16bf Wq[2][8], Wk[2][8], Wv[2][8];
  {
    const int srow = l >> 4, cp = l & 15;
    auto stage = [&](int t, int buf) {
#pragma unroll
      for (int m = 0; m < 3; ++m) {
        const u16* W = m == 0 ? p.wq : (m == 1 ? p.wk : p.wv);
#pragma unroll
        for (int n = 0; n < 4; ++n) {
          const int r = 16 * w + 4 * n + srow;
          const int ch = cp ^ (2 * (r & 7));
          glds16(W + (size_t)(64 * t + r) * 4096 + h * 128 + ch * 8, ldsb + buf * 49152 + m * 16384 + (16 * w + 4 * n) * 256);
        }
      }
    };
    const int gb = l >> 4, q = (l & 15) >> 2, pp = l & 3;
    const int rr = 16 * (gb >> 1) + 4 * (gb & 1) + q;
    u32 rb[2];
#pragma unroll
    for (int T = 0; T < 2; ++T) { const int ch = 4 * w + 2 * T + (pp >> 1); rb[T] = rr * 256 + ((ch ^ (2 * (rr & 7))) * 16) + (pp & 1) * 8; }
    stage(0, 0); stage(1, 1); stage(2, 2);
#pragma unroll
    for (int t = 0; t < 8; ++t) {
      if (t < 6) asm volatile("s_waitcnt vmcnt(24)" ::: "memory"); else if (t == 6) asm volatile("s_waitcnt vmcnt(12)" ::: "memory"); else wait_vm0();
      bar();
      if (t == 0 && tid == 0) __hip_atomic_fetch_add(ctrl + C_HINIT, 1u, __ATOMIC_RELAXED, __HIP_MEMORY_SCOPE_AGENT);
      const u32 b = (t % 3) * 49152;
#pragma unroll
      for (int T = 0; T < 2; ++T) {
        Wq[T][t] = read_bfrag(lds, b + rb[T]); Wk[T][t] = read_bfrag(lds, b + 16384 + rb[T]); Wv[T][t] = read_bfrag(lds, b + 32768 + rb[T]);
        asm volatile("" : "+a"(Wq[T][t]), "+a"(Wk[T][t]), "+v"(Wv[T][t]));
      }
      bar();
      if (t + 3 < 8) stage(t + 3, t % 3);
      else if (t == 5) {
#pragma unroll
        for (int t2 = 0; t2 < 2; ++t2)
#pragma unroll
          for (int n = 0; n < 4; ++n) {
            const int r = 16 * w + 4 * n + srow;
            const int ch = cp ^ (2 * (r & 7));
            glds16(p.wo + (size_t)(h * 128 + 64 * t2 + r) * 512 + c * 128 + ch * 8, ldsb + (t % 3) * 49152 + t2 * 16384 + (16 * w + 4 * n) * 256);
          }
      }
    }
    {
      v16bf wo[2][2];
#pragma unroll
      for (int T = 0; T < 2; ++T)
#pragma unroll
        for (int t2 = 0; t2 < 2; ++t2) wo[T][t2] = read_bfrag(lds, 2 * 49152 + t2 * 16384 + rb[T]);
      bar();
#pragma unroll
      for (int T = 0; T < 2; ++T)
#pragma unroll
        for (int t2 = 0; t2 < 2; ++t2) {
          const v8u x = __builtin_bit_cast(v8u, wo[T][t2]);
          LAS char* d = (LAS char*)(lds + H_WO + w * 8192 + (T * 2 + t2) * 2048 + l * 16);
          *(LAS v4u*)d = (v4u){x[0], x[1], x[2], x[3]}; *(LAS v4u*)(d + 1024) = (v4u){x[4], x[5], x[6], x[7]};
        }
    }
    bar();
  }
  DSTAMP(1);
  if (hidx == 0 && w < 2) {
    char* base = comm + (w ? 3 * MiB : 0);
    u32 best[2] = {~0u, ~0u};
#pragma unroll 1
    for (int r = 0; r < 6; ++r) {
      char* a = base + (r & 1) * 8192;
      st_sc1(a, ~0u); wait_vm0();
      const u32 t0 = clk(); const u32 v = ld_sc1(a); const u32 t1 = clk();
      asm volatile("" :: "v"(v));
      const u32 d = t1 - t0;
      if (r & 1) best[1] = min(best[1], d); else best[0] = min(best[0], d);
    }
    if (l == 0) st_sc1(ctrl + (w ? C_PAR1 : C_PAR0), best[0] <= best[1] ? 1u : 2u);
  }
  {
    const int row = tid & 7, ch = tid >> 3;
#pragma unroll
    for (int k = 0; k < 2; ++k) {
      const v4u v = *(const v4u*)(p.mem0 + row * 512 + (ch + 32 * k) * 8);
      *(LAS v4u*)(LAS char*)(lds + H_MBUF + (ch + 32 * k) * 128 + row * 16) = v;
    }
  }
  const char* kxg = p.ws + WS_KX + (size_t)h * 8192;
  const char* vxg = p.ws + WS_VX + (size_t)h * 8192;
  u32 kvready = 0;
  auto fetch_kv = [&](int s) {
    if (w >= 2) {
      if (kvready <= (u32)s) kvready = wait_ge(ctrl, w == 2 ? C_PROGK(h) : C_PROGV(h), s + 1);
      const char* g = (w == 2 ? kxg : vxg) + (size_t)s * (32 * 8192) + l * 16;
      v4u t0, t1, t2, t3, t4, t5, t6, t7;
      asm volatile("global_load_dwordx4 %0, %8, off sc1\n\tglobal_load_dwordx4 %1, %8, off offset:1024 sc1\n\tglobal_load_dwordx4 %2, %8, off offset:2048 sc1\n\tglobal_load_dwordx4 %3, %8, off offset:3072 sc1\n\t"
                   "global_load_dwordx4 %4, %9, off sc1\n\tglobal_load_dwordx4 %5, %9, off offset:1024 sc1\n\tglobal_load_dwordx4 %6, %9, off offset:2048 sc1\n\tglobal_load_dwordx4 %7, %9, off offset:3072 sc1\n\ts_waitcnt vmcnt(0)"
                   : "=&v"(t0), "=&v"(t1), "=&v"(t2), "=&v"(t3), "=&v"(t4), "=&v"(t5), "=&v"(t6), "=&v"(t7) : "v"(g), "v"(g + 4096) : "memory");
      const u32 st = w == 2 ? 1024u : 4u * VX_STRIDE;
      LAS char* d = (LAS char*)(lds + (w == 2 ? H_KX + l * 16 : H_VX + (l >> 4) * VX_STRIDE + (l & 15) * 16));
      *(LAS v4u*)(d) = t0; *(LAS v4u*)(d + st) = t1; *(LAS v4u*)(d + 2 * st) = t2; *(LAS v4u*)(d + 3 * st) = t3;
      *(LAS v4u*)(d + 4 * st) = t4; *(LAS v4u*)(d + 5 * st) = t5; *(LAS v4u*)(d + 6 * st) = t6; *(LAS v4u*)(d + 7 * st) = t7;
    }
  };
  fetch_kv(0);
  DSTAMP(2);
  wait_ge(ctrl, C_HINIT, 128);
  const u32 par0 = wait_ge(ctrl, C_PAR0, 1) - 1u, par1 = wait_ge(ctrl, C_PAR1, 1) - 1u;
  char* const mh = comm + (par0 & 1u) * 8192;
  char* const slot0 = comm + MiB + (par0 & 1u) * 8192;
  char* const slot1 = comm + 2 * MiB + (par1 & 1u) * 8192;

  const int idx = (l & 8) ? 0xEEEE : 0x4444;
  const int g = l >> 4, hh = g & 1;
  const u32 a_off = l * 16;
  const u32 img_w = (32 * w + 16 * (l >> 5) + (l & 15)) * 16 + hh * 8;
  const u32 img_r = (8 * g + ((l & 15) >> 2)) * 16 + (l & 1) * 8;
  const u32 kx_r = H_KX + l * 16;
  const u32 vx_r = H_VX + (4 * g + ((l & 15) >> 2)) * VX_STRIDE + (32 * w + 4 * (l & 3)) * 2;
  const u32 ob_w = H_OB + (4 * w + 2 * ((l >> 3) & 1) + (g >> 1)) * 128 + (l & 7) * 16 + (g & 1) * 8;
  const float fm = g < 2 ? 1.f : 0.f;
  u32 pst;
  { const int T = 8 * c + 2 * w + (l >> 5), col = l & 15; pst = rwoff((2 * T + (col >> 3)) * 2 + hh) + h * 128 + (col & 7) * 16; }
  const u32 gat = rwoff(hidx) + l * 16;
  const u32 mst = (hidx >> 1) * 128 + (4 * (hidx & 1) + g) * 16 + (l & 7) * 2;
  const __amdgpu_buffer_rsrc_t rk = __builtin_amdgcn_make_buffer_rsrc(p.ws + WS_KM, 0, c == 0 ? 4u * (u32)MiB : 0u, 0x00020000);
  const __amdgpu_buffer_rsrc_t rv = __builtin_amdgcn_make_buffer_rsrc(p.ws + WS_VM, 0, c == 1 ? 4u * (u32)MiB : 0u, 0x00020000);
  const u32 kvm_off = h * 2048 + img_w;
  const u32 wo_r = H_WO + w * 8192 + l * 16;
  const bool up = (l & 8) != 0;
  constexpr float CE = 0.12751743082459868f;
  v4f q0 = {0, 0, 0, 0}, q1 = q0, k0 = q0, k1 = q0, v0 = q0, v1 = q0;
  asm volatile("" : "+v"(q0), "+v"(q1), "+v"(k0), "+v"(k1), "+v"(v0), "+v"(v1));

#pragma unroll 1
  for (int s = 0; s < 64; ++s) {
    bar();
    DSEG(0);
    v8bf a[8];
#pragma unroll
    for (int t = 0; t < 8; ++t) a[t] = *(LAS v8bf*)(LAS char*)(lds + H_MBUF + t * 1024 + a_off);
#pragma unroll
    for (int t = 0; t < 8; ++t) {
      q0 = smf(a[t], Wq[0][t], q0, idx); q1 = smf(a[t], Wq[1][t], q1, idx);
      k0 = smf(a[t], Wk[0][t], k0, idx); k1 = smf(a[t], Wk[1][t], k1, idx);
    }
    asm volatile("" : "+v"(q0), "+v"(q1), "+v"(k0), "+v"(k1), "+v"(v0), "+v"(v1));
    DSEG(1);
#pragma unroll
    for (int t = 0; t < 4; ++t) { v0 = smf(a[t], Wv[0][t], v0, idx); v1 = smf(a[t], Wv[1][t], v1, idx); }
    const v4f qs = half_sum(q0, q1), ks = half_sum(k0, k1);
    const v2u qp = {pack2(qs[0], qs[1]), pack2(qs[2], qs[3])}, kp = {pack2(ks[0], ks[1]), pack2(ks[2], ks[3])};
    *(LAS v2u*)(LAS char*)(lds + H_QT + img_w) = qp;
    *(LAS v2u*)(LAS char*)(lds + H_KT + img_w) = kp;
    asm volatile("" : "+v"(v0), "+v"(v1));
    bar();
    DSEG(2);
    v8bf qf[4], kmf[4];
#pragma unroll
    for (int kk = 0; kk < 4; ++kk) {
      const v2u x0 = tr_read(lds, H_QT + kk * 512 + img_r), x1 = tr_read(lds, H_QT + kk * 512 + 64 + img_r);
      const v2u y0 = tr_read(lds, H_KT + kk * 512 + img_r), y1 = tr_read(lds, H_KT + kk * 512 + 64 + img_r);
      const v4u xq = {x0[0], x0[1], x1[0], x1[1]}, yk = {y0[0], y0[1], y1[0], y1[1]};
      qf[kk] = __builtin_bit_cast(v8bf, xq); kmf[kk] = __builtin_bit_cast(v8bf, yk);
    }
    v8bf kx0[4], kx1[4];
#pragma unroll
    for (int kk = 0; kk < 4; ++kk) {
      kx0[kk] = *(LAS v8bf*)(LAS char*)(lds + kx_r + kk * 1024);
      kx1[kk] = *(LAS v8bf*)(LAS char*)(lds + kx_r + 4096 + kk * 1024);
    }
#pragma unroll
    for (int t = 4; t < 8; ++t) { v0 = smf(a[t], Wv[0][t], v0, idx); v1 = smf(a[t], Wv[1][t], v1, idx); }
    asm volatile("" : "+v"(v0), "+v"(v1));
    v4f sm = {0, 0, 0, 0}, s0 = sm, s1 = sm;
#pragma unroll
    for (int kk = 0; kk < 4; ++kk) { s0 = mfma(kx0[kk], qf[kk], s0); s1 = mfma(kx1[kk], qf[kk], s1); sm = mfma(kmf[kk], qf[kk], sm); }
    const v4f vs = half_sum(v0, v1);
    const v2u vp = {pack2(vs[0], vs[1]), pack2(vs[2], vs[3])};
    __builtin_amdgcn_raw_buffer_store_b64(kp, rk, kvm_off, s * 65536, 16);
    __builtin_amdgcn_raw_buffer_store_b64(vp, rv, kvm_off, s * 65536, 16);
    if (s == 63) break;
    DSEG(3);
    v8bf vx[2], vm[2];
#pragma unroll
    for (int T = 0; T < 2; ++T) {
      const v2u x0 = tr_read(lds, vx_r + T * 32), x1 = tr_read(lds, vx_r + T * 32 + 16 * VX_STRIDE);
      const v4u t = {x0[0], x0[1], x1[0], x1[1]};
      vx[T] = __builtin_bit_cast(v8bf, t);
    }
    v16bf wo[2][2];
#pragma unroll
    for (int T = 0; T < 2; ++T)
#pragma unroll
      for (int t2 = 0; t2 < 2; ++t2) {
        const v4u x = *(LAS v4u*)(LAS char*)(lds + wo_r + (T * 2 + t2) * 2048), y = *(LAS v4u*)(LAS char*)(lds + wo_r + (T * 2 + t2) * 2048 + 1024);
        const v8u z = {x[0], x[1], x[2], x[3], y[0], y[1], y[2], y[3]};
        wo[T][t2] = __builtin_bit_cast(v16bf, z);
      }
    {
      auto e0 = __builtin_amdgcn_permlane32_swap(vp[0], vp[0], false, false);
      auto e1 = __builtin_amdgcn_permlane32_swap(vp[1], vp[1], false, false);
      const v4u t0 = {e0[0], e1[0], e0[0], e1[0]}, t1 = {e0[1], e1[1], e0[1], e1[1]};
      vm[0] = __builtin_bit_cast(v8bf, t0); vm[1] = __builtin_bit_cast(v8bf, t1);
    }
    s0 *= CE; s1 *= CE; sm *= CE;
    float mx = fmaxf(fmaxf(fmaxf(s0[0], s0[1]), s0[2]), fmaxf(fmaxf(s0[3], s1[0]), s1[1]));
    mx = fmaxf(fmaxf(fmaxf(s1[2], s1[3]), mx), fmaxf(fmaxf(sm[0], sm[1]), fmaxf(sm[2], sm[3])));
    mx = xmax32(xmax16(mx));
    v4f em, e0v, e1v;
#pragma unroll
    for (int i = 0; i < 4; ++i) {
      e0v[i] = __builtin_amdgcn_exp2f(s0[i] - mx); e1v[i] = __builtin_amdgcn_exp2f(s1[i] - mx);
      em[i] = __builtin_amdgcn_exp2f(sm[i] - mx);
    }
    em *= fm;
    const v4u px = {pack2(e0v[0], e0v[1]), pack2(e0v[2], e0v[3]), pack2(e1v[0], e1v[1]), pack2(e1v[2], e1v[3])};
    const v4u pm = {pack2(em[0], em[1]), pack2(em[2], em[3]), 0u, 0u};
    v4f o0 = {0, 0, 0, 0}, o1 = o0;
    o0 = mfma(vx[0], __builtin_bit_cast(v8bf, px), o0); o1 = mfma(vx[1], __builtin_bit_cast(v8bf, px), o1);
    o0 = mfma(vm[0], __builtin_bit_cast(v8bf, pm), o0); o1 = mfma(vm[1], __builtin_bit_cast(v8bf, pm), o1);
    const v4f es = (e0v + e1v) + em;
    const float sum = xsum32(xsum16((es[0] + es[1]) + (es[2] + es[3])));
    const float inv = __builtin_amdgcn_rcpf(sum);
    v4f y0 = {0, 0, 0, 0}, y1 = y0;
    asm volatile("" : "+v"(y0), "+v"(y1));
    {
      const v4f o = up ? o1 : o0;
      const v2u op = {pack2(o[0] * inv, o[1] * inv), pack2(o[2] * inv, o[3] * inv)};
      *(LAS v2u*)(LAS char*)(lds + ob_w) = op;
    }
    char* slot = (s & 1) ? slot1 : slot0;
    char* pdst = slot + pst;
    asm volatile("" : "+v"(pdst));
    bar();
    DSEG(4);
    const v8bf oa0 = *(LAS v8bf*)(LAS char*)(lds + H_OB + a_off), oa1 = *(LAS v8bf*)(LAS char*)(lds + H_OB + 1024 + a_off);
    y0 = smf(oa0, wo[0][0], y0, idx); y1 = smf(oa0, wo[1][0], y1, idx);
    y0 = smf(oa1, wo[0][1], y0, idx); y1 = smf(oa1, wo[1][1], y1, idx);
    const v4f ys = half_sum(y0, y1);
    st16_sc1(pdst, __builtin_bit_cast(v4u, ys));
    const u32 tpub = clk();
    DSEG(5);
    q0 = (v4f){0, 0, 0, 0}; q1 = q0; k0 = q0; k1 = q0; v0 = q0; v1 = q0;
    asm volatile("" : "+v"(q0), "+v"(q1), "+v"(k0), "+v"(k1), "+v"(v0), "+v"(v1));
    char* mnext = mh + (size_t)(s + 1) * 16384;
    u32 tg;
    if (w == 0) {
      char* base = slot + gat;
      char* mdst = mnext + mst;
      asm volatile("" : "+v"(base), "+v"(mdst));
      wait_until(tpub + D1_CLK);
      float t;
      u32 n = 0;
      while (true) {
        v4f g0, g1, g2, g3;
        asm volatile("global_load_dwordx4 %0, %4, off sc1\n\tglobal_load_dwordx4 %1, %4, off offset:1024 sc1\n\tglobal_load_dwordx4 %2, %4, off offset:2048 sc1\n\tglobal_load_dwordx4 %3, %4, off offset:3072 sc1\n\ts_waitcnt vmcnt(0)"
                     : "=&v"(g0), "=&v"(g1), "=&v"(g2), "=&v"(g3) : "v"(base) : "memory");
        const v4f sv = (g0 + g1) + (g2 + g3);
        auto aa = __builtin_amdgcn_permlane32_swap(f2u(sv[0]), f2u(sv[2]), false, false);
        auto bb = __builtin_amdgcn_permlane32_swap(f2u(sv[1]), f2u(sv[3]), false, false);
        const float x0 = u2f(aa[0]) + u2f(aa[1]), x1 = u2f(bb[0]) + u2f(bb[1]);
        auto cc = __builtin_amdgcn_permlane16_swap(f2u(x0), f2u(x1), false, false);
        t = u2f(cc[0]) + u2f(cc[1]);
        t += u2f(__builtin_amdgcn_update_dpp(0, f2u(t), 0x128, 0xf, 0xf, false));
        if (!__any(t != t)) break;
        const v4u m0 = __builtin_elementwise_max(__builtin_elementwise_max(__builtin_bit_cast(v4u, g0), __builtin_bit_cast(v4u, g1)),
                                                 __builtin_elementwise_max(__builtin_bit_cast(v4u, g2), __builtin_bit_cast(v4u, g3)));
        if (!__any(max(max(m0[0], m0[1]), max(m0[2], m0[3])) == ~0u)) break;
        DCOUNT(dr1);
        if (spin_fail(n, ctrl)) break;
      }
      DSEG(6);
      const float nbv = u2f(__builtin_amdgcn_update_dpp(0, f2u(t), 0xB1, 0xf, 0xf, false));
      u32 pk = pack2(t, nbv);
      pk = pk == ~0u ? 0x7FFF7FFFu : pk;
      if ((l & 9) == 0) st_sc1(mdst, pk);
      tg = clk() + D2_CLK;
      const v4u ones = {~0u, ~0u, ~0u, ~0u};
      asm volatile("global_store_dwordx4 %0, %1, off sc1\n\tglobal_store_dwordx4 %0, %1, off offset:1024 sc1\n\tglobal_store_dwordx4 %0, %1, off offset:2048 sc1\n\tglobal_store_dwordx4 %0, %1, off offset:3072 sc1\n\ts_nop 1"
                   :: "v"(base), "v"(ones) : "memory");
    } else {
      if (w == 1 && c < 2 && s > 0 && l == 0) st_sc1(ctrl + (c == 0 ? C_PROGKM(h) : C_PROGVM(h)), s);
      fetch_kv(s + 1);
      tg = tpub + DX_CLK;
    }
    {
      char* mp = mnext + w * 2048 + l * 16;
      asm volatile("" : "+v"(mp));
      wait_until(tg);
      v4u m0, m1; u32 n = 0;
      while (true) {
        asm volatile("global_load_dwordx4 %0, %2, off sc1\n\tglobal_load_dwordx4 %1, %2, off offset:1024 sc1\n\ts_waitcnt vmcnt(0)" : "=&v"(m0), "=&v"(m1) : "v"(mp) : "memory");
        const v4u mm = __builtin_elementwise_max(m0, m1);
        if (!__any(max(max(mm[0], mm[1]), max(mm[2], mm[3])) == ~0u)) break;
        DCOUNT(dr2);
        if (spin_fail(n, ctrl)) break;
      }
      DSEG(7);
      *(LAS v4u*)(LAS char*)(lds + H_MBUF + w * 2048 + l * 16) = m0;
      *(LAS v4u*)(LAS char*)(lds + H_MBUF + w * 2048 + 1024 + l * 16) = m1;
    }
  }
  DSTAMP(3);
  wait_vm0();
  bar();
  if (c < 2 && tid == 0) st_sc1(ctrl + (c == 0 ? C_PROGKM(h) : C_PROGVM(h)), 64u);
}

constexpr u32 A_STRIDE = 1040, A_BUF = 32 * A_STRIDE;
constexpr u32 P_CBUF = 2 * A_BUF, C_STRIDE = 272, C_BUF = 32 * C_STRIDE;

__device__ __forceinline__ void load_stationary(v8bf (&W)[2][16], const u16* gw, char* lds, const int l, const int w) {
  const u32 ldsb = (u32)(size_t)(LAS char*)lds;
  const int srow = l >> 4, cp = l & 15;
#pragma unroll 1
  for (int n = 0; n < 32; ++n) {
    const int r = 128 * w + 4 * n + srow;
    const int ch = cp ^ (2 * (r & 7));
    glds16(gw + (size_t)r * 4096 + ch * 8, ldsb + (128 * w + 4 * n) * 256);
  }
  wait_vm0();
  bar();
  const int g = l >> 4, q = (l & 15) >> 2, pp = l & 3;
#pragma unroll
  for (int T = 0; T < 2; ++T) {
    const int ch = 4 * w + 2 * T + (pp >> 1);
    const int r0 = 8 * g + q, r1 = r0 + 4;
    const u32 b0 = r0 * 256 + ((ch ^ (2 * (r0 & 7))) * 16) + (pp & 1) * 8, b1 = r1 * 256 + ((ch ^ (2 * (r1 & 7))) * 16) + (pp & 1) * 8;
#pragma unroll
    for (int kk = 0; kk < 16; ++kk) {
      const v2u x0 = tr_read(lds, b0 + kk * 8192), x1 = tr_read(lds, b1 + kk * 8192);
      const v4u t = {x0[0], x0[1], x1[0], x1[1]};
      W[T][kk] = __builtin_bit_cast(v8bf, t);
    }
  }
  bar();
}

template <int NM>
__device__ __forceinline__ void proj_role(const Params& p, char* lds, const int h, const int slotid) {
  const int tid = threadIdx.x, l = tid & 63, w = rfl(tid >> 6);
  u32* ctrl = (u32*)p.ws;
  const u32 ldsb = (u32)(size_t)(LAS char*)lds;
  v8bf W[NM][2][16];
  load_stationary(W[0], (NM == 2 ? p.wk : p.wv) + h * 128, lds, l, w);
  if constexpr (NM == 2) load_stationary(W[1], p.wq + h * 128, lds, l, w);
  auto stage = [&](int s, int buf) {
#pragma unroll
    for (int n = 0; n < 8; ++n) { const int r = 8 * w + n; glds16(p.bx + (size_t)(32 * s + r) * 512 + l * 8, ldsb + buf * A_BUF + r * A_STRIDE); }
  };
  u32* prog = ctrl + (NM == 2 ? C_PROGK(h) : C_PROGV(h));
  WSTAMP(1);
  stage(0, 0);
#define PSEG(k) do {} while (0)
#pragma unroll 1
  for (int s = 0; s < 64; ++s) {
    if (s <= 1) wait_vm0(); else if (NM == 2) asm volatile("s_waitcnt vmcnt(4)" ::: "memory"); else asm volatile("s_waitcnt vmcnt(2)" ::: "memory");
    bar();
    if (s == 1 || s == 17 || s == 33) WSTAMP(2 + (s >> 4));
    PSEG(0);
    if (s == 1 && tid == 0) st_sc1(prog, 1u);
    if (s + 1 < 64) stage(s + 1, (s + 1) & 1);
    v4f acc[NM][2][2];
#pragma unroll
    for (int m = 0; m < NM; ++m)
#pragma unroll
      for (int tt = 0; tt < 2; ++tt) { acc[m][tt][0] = (v4f){0, 0, 0, 0}; acc[m][tt][1] = (v4f){0, 0, 0, 0}; }
    const u32 ar = (s & 1) * A_BUF + (l & 15) * A_STRIDE + (l >> 4) * 16;
    v8bf xa[2][4], xb[2][4];
    auto lda = [&](v8bf (&x)[2][4], const int k0) {
#pragma unroll
      for (int k = 0; k < 4; ++k)
#pragma unroll
        for (int tt = 0; tt < 2; ++tt) x[tt][k] = *(LAS v8bf*)(LAS char*)(lds + ar + tt * 16 * A_STRIDE + (k0 + k) * 64);
    };
    auto mma = [&](const v8bf (&x)[2][4], const int k0) {
#pragma unroll
      for (int k = 0; k < 4; ++k)
#pragma unroll
        for (int tt = 0; tt < 2; ++tt)
#pragma unroll
          for (int m = 0; m < NM; ++m) { acc[m][tt][0] = mfma(W[m][0][k0 + k], x[tt][k], acc[m][tt][0]); acc[m][tt][1] = mfma(W[m][1][k0 + k], x[tt][k], acc[m][tt][1]); }
    };
    auto mix = [&]() {
#pragma unroll
      for (int i = 0; i < 8; ++i) { __builtin_amdgcn_sched_group_barrier(0x008, 2 * NM, 0); __builtin_amdgcn_sched_group_barrier(0x100, 1, 0); }
      __builtin_amdgcn_sched_barrier(0);
    };
    lda(xa, 0); __builtin_amdgcn_sched_barrier(0);
    lda(xb, 4); mma(xa, 0); mix();
    lda(xa, 8); mma(xb, 4); mix();
    lda(xb, 12); mma(xa, 8); mix();
    mma(xb, 12);
#pragma unroll
    for (int m = 0; m < NM; ++m)
#pragma unroll
      for (int tt = 0; tt < 2; ++tt)
#pragma unroll
        for (int T = 0; T < 2; ++T) {
          const v2u v = {pack2(acc[m][tt][T][0], acc[m][tt][T][1]), pack2(acc[m][tt][T][2], acc[m][tt][T][3])};
          *(LAS v2u*)(LAS char*)(lds + P_CBUF + m * C_BUF + (16 * tt + (l & 15)) * C_STRIDE + (32 * w + 16 * T + 4 * (l >> 4)) * 2) = v;
        }
    PSEG(1);
    if (s + 1 < 64) asm volatile("s_waitcnt vmcnt(8)" ::: "memory"); else wait_vm0();
    bar();
    PSEG(2);
    if (s > 1 && tid == 0) st_sc1(prog, s);
#pragma unroll
    for (int m = 0; m < NM; ++m) {
      char* dst = p.ws + (NM == 1 ? WS_VX : (m == 0 ? WS_KX : WS_QX)) + (size_t)(s * 32 + h) * 8192;
#pragma unroll
      for (int i = 0; i < 2; ++i) {
        if (NM == 1) {
          const int r = 8 * w + 4 * i + (l >> 4);
          const v4u v = *(LAS v4u*)(LAS char*)(lds + P_CBUF + r * C_STRIDE + (l & 15) * 16);
          st16_sc1(dst + r * 256 + (l & 15) * 16, v);
        } else {
          const int t = w >> 1, kk = 2 * (w & 1) + i;
          const v4u v = *(LAS v4u*)(LAS char*)(lds + P_CBUF + m * C_BUF + (16 * t + (l & 15)) * C_STRIDE + (32 * kk + 8 * (l >> 4)) * 2);
          st16_sc1(dst + (t * 4 + kk) * 1024 + l * 16, v);
        }
      }
    }
    PSEG(3);
  }
  wait_vm0();
  bar();
  if (tid == 0) st_sc1(prog, 64u);
  WSTAMP(7);
}

constexpr u32 AT_V = 0, AT_KM = 32 * 288, AT_WAVE = AT_KM + 2048;
__device__ __forceinline__ v4u ldb16(__amdgpu_buffer_rsrc_t r, u32 voff, u32 soff) { return __builtin_bit_cast(v4u, __builtin_amdgcn_raw_buffer_load_b128(r, voff, soff, 16)); }
__device__ __forceinline__ v2u ldb8(__amdgpu_buffer_rsrc_t r, u32 voff, u32 soff) { return __builtin_bit_cast(v2u, __builtin_amdgcn_raw_buffer_load_b64(r, voff, soff, 16)); }

__device__ __forceinline__ void attn_role(const Params& p, char* lds0, const int h, const int slotid) {
  const int tid = threadIdx.x, l = tid & 63, w = rfl(tid >> 6);
  u32* ctrl = (u32*)p.ws;
  char* lds = lds0 + w * AT_WAVE;
  const int g = l >> 4, c16 = l & 15;
  const __amdgpu_buffer_rsrc_t rq = __builtin_amdgcn_make_buffer_rsrc(p.ws + WS_QX, 0, 16u * (u32)MiB, 0x00020000);
  const __amdgpu_buffer_rsrc_t rk = __builtin_amdgcn_make_buffer_rsrc(p.ws + WS_KX, 0, 16u * (u32)MiB, 0x00020000);
  const __amdgpu_buffer_rsrc_t rv = __builtin_amdgcn_make_buffer_rsrc(p.ws + WS_VX, 0, 16u * (u32)MiB, 0x00020000);
  const __amdgpu_buffer_rsrc_t rkm = __builtin_amdgcn_make_buffer_rsrc(p.ws + WS_KM, 0, 4u * (u32)MiB, 0x00020000);
  const __amdgpu_buffer_rsrc_t rvm = __builtin_amdgcn_make_buffer_rsrc(p.ws + WS_VM, 0, 4u * (u32)MiB, 0x00020000);
  const __amdgpu_buffer_rsrc_t ro = __builtin_amdgcn_make_buffer_rsrc(p.ws + WS_O, 0, 16u * (u32)MiB, 0x00020000);
  const u32 img_r = (8 * g + (c16 >> 2)) * 16 + (l & 1) * 8;
  const u32 vx_r = AT_V + (4 * g + (c16 >> 2)) * 288 + 4 * (l & 3) * 2;
  const float fm = g < 2 ? 1.f : 0.f;
  constexpr float CE = 0.12751743082459868f;
  u32 rdyK = 0, rdyV = 0, rdyKM = 0, rdyVM = 0;
#pragma unroll 1
  for (int s = w; s < 64; s += 4) {
    if (rdyK <= (u32)s) rdyK = wait_ge(ctrl, C_PROGK(h), s + 1);
    if (rdyV <= (u32)s) rdyV = wait_ge(ctrl, C_PROGV(h), s + 1);
    asm volatile("" ::: "memory");
    const u32 tile = (s * 32 + h) * 8192;
    v8bf qf[2][4], kf[2][4];
#pragma unroll
    for (int t = 0; t < 2; ++t)
#pragma unroll
      for (int kk = 0; kk < 4; ++kk) {
        qf[t][kk] = __builtin_bit_cast(v8bf, ldb16(rq, l * 16 + (t * 4 + kk) * 1024, tile));
        kf[t][kk] = __builtin_bit_cast(v8bf, ldb16(rk, l * 16 + (t * 4 + kk) * 1024, tile));
      }
#pragma unroll
    for (int n = 0; n < 8; ++n) {
      const v4u v = ldb16(rv, l * 16 + n * 1024, tile);
      *(LAS v4u*)(LAS char*)(lds + AT_V + (4 * n + g) * 288 + c16 * 16) = v;
    }
    v4f s00 = {0, 0, 0, 0}, s10 = s00, s11 = s00;
#pragma unroll
    for (int kk = 0; kk < 4; ++kk) { s00 = mfma(kf[0][kk], qf[0][kk], s00); s10 = mfma(kf[0][kk], qf[1][kk], s10); s11 = mfma(kf[1][kk], qf[1][kk], s11); }
    if (rdyKM <= (u32)s) rdyKM = wait_ge(ctrl, C_PROGKM(h), s + 1);
    if (rdyVM <= (u32)s) rdyVM = wait_ge(ctrl, C_PROGVM(h), s + 1);
    asm volatile("" ::: "memory");
    const u32 mt = (s * 32 + h) * 2048;
    {
      const v4u k0 = ldb16(rkm, l * 16, mt), k1 = ldb16(rkm, l * 16 + 1024, mt);
      *(LAS v4u*)(LAS char*)(lds + AT_KM + l * 16) = k0; *(LAS v4u*)(LAS char*)(lds + AT_KM + 1024 + l * 16) = k1;
    }
    v2u vmr[8];
#pragma unroll
    for (int E = 0; E < 8; ++E) vmr[E] = ldb8(rvm, (16 * E + c16) * 16 + (g & 1) * 8, mt);
    wait_lgkm0();
    v4f sm0 = {0, 0, 0, 0}, sm1 = sm0;
#pragma unroll
    for (int kk = 0; kk < 4; ++kk) {
      const v2u y0 = tr_read(lds, AT_KM + kk * 512 + img_r), y1 = tr_read(lds, AT_KM + kk * 512 + 64 + img_r);
      const v4u yk = {y0[0], y0[1], y1[0], y1[1]};
      const v8bf km = __builtin_bit_cast(v8bf, yk);
      sm0 = mfma(km, qf[0][kk], sm0); sm1 = mfma(km, qf[1][kk], sm1);
    }
    v8bf vx[8];
#pragma unroll
    for (int E = 0; E < 8; ++E) {
      const v2u x0 = tr_read(lds, vx_r + E * 32), x1 = tr_read(lds, vx_r + E * 32 + 16 * 288);
      const v4u t = {x0[0], x0[1], x1[0], x1[1]};
      vx[E] = __builtin_bit_cast(v8bf, t);
    }
#pragma unroll
    for (int qt = 0; qt < 2; ++qt) {
      v4f a = qt ? s10 : s00, b = qt ? s11 : s00, m = qt ? sm1 : sm0;
      a *= CE; b *= CE; m *= CE;
      constexpr float NEG = -1e30f;
#pragma unroll
      for (int i = 0; i < 4; ++i) {
        const bool vis = 4 * g + i <= c16;
        if (qt == 0) { a[i] = vis ? a[i] : NEG; b[i] = NEG; } else b[i] = vis ? b[i] : NEG;
      }
      float mx = fmaxf(fmaxf(fmaxf(a[0], a[1]), fmaxf(a[2], a[3])), fmaxf(fmaxf(m[0], m[1]), fmaxf(m[2], m[3])));
      if (qt) mx = fmaxf(mx, fmaxf(fmaxf(b[0], b[1]), fmaxf(b[2], b[3])));
      mx = xmax32(xmax16(mx));
      v4f ea, eb, em;
#pragma unroll
      for (int i = 0; i < 4; ++i) {
        ea[i] = __builtin_amdgcn_exp2f(a[i] - mx); eb[i] = qt ? __builtin_amdgcn_exp2f(b[i] - mx) : 0.f;
        em[i] = __builtin_amdgcn_exp2f(m[i] - mx) * fm;
      }
      const v4f es = (ea + eb) + em;
      const float inv = __builtin_amdgcn_rcpf(xsum32(xsum16((es[0] + es[1]) + (es[2] + es[3]))));
      ea *= inv; eb *= inv; em *= inv;
      const v4u px = {pack2(ea[0], ea[1]), pack2(ea[2], ea[3]), pack2(eb[0], eb[1]), pack2(eb[2], eb[3])};
      const v4u pm = {pack2(em[0], em[1]), pack2(em[2], em[3]), 0u, 0u};
#pragma unroll
      for (int E = 0; E < 8; E += 2) {
        v4f o0 = {0, 0, 0, 0}, o1 = o0;
        const v4u m0 = {vmr[E][0], vmr[E][1], vmr[E][0], vmr[E][1]}, m1 = {vmr[E + 1][0], vmr[E + 1][1], vmr[E + 1][0], vmr[E + 1][1]};
        o0 = mfma(vx[E], __builtin_bit_cast(v8bf, px), o0); o1 = mfma(vx[E + 1], __builtin_bit_cast(v8bf, px), o1);
        o0 = mfma(__builtin_bit_cast(v8bf, m0), __builtin_bit_cast(v8bf, pm), o0); o1 = mfma(__builtin_bit_cast(v8bf, m1), __builtin_bit_cast(v8bf, pm), o1);
        auto x0 = __builtin_amdgcn_permlane16_swap(pack2(o0[0], o0[1]), pack2(o1[0], o1[1]), false, false);
        auto x1 = __builtin_amdgcn_permlane16_swap(pack2(o0[2], o0[3]), pack2(o1[2], o1[3]), false, false);
        const v4u ov = {x0[0], x1[0], x0[1], x1[1]};
        __builtin_amdgcn_raw_buffer_store_b128(ov, ro, ((2 * (g & 1) + (g >> 1)) * 16 + c16) * 16, ((s * 2 + qt) * 128 + 4 * h + E / 2) * 1024, 16);
      }
    }
    wait_vm0();
    if (l == 0) st_sc1(ctrl + C_PROGO(h, w), (s >> 2) + 1);
    if (s == 0 || s == 16 || s == 32) WSTAMP(2 + (s >> 4));
  }
  WSTAMP(7);
}

__device__ __forceinline__ void op_issue(v4u (&x)[2][8], const u32 voff, const char* sb) {
  asm volatile(
      "global_load_dwordx4 %0, %16, %20 sc1\n\tglobal_load_dwordx4 %8, %17, %20 sc1\n\t"
      "global_load_dwordx4 %1, %16, %20 offset:1024 sc1\n\tglobal_load_dwordx4 %9, %17, %20 offset:1024 sc1\n\t"
      "global_load_dwordx4 %2, %16, %20 offset:2048 sc1\n\tglobal_load_dwordx4 %10, %17, %20 offset:2048 sc1\n\t"
      "global_load_dwordx4 %3, %16, %20 offset:3072 sc1\n\tglobal_load_dwordx4 %11, %17, %20 offset:3072 sc1\n\t"
      "global_load_dwordx4 %4, %18, %20 sc1\n\tglobal_load_dwordx4 %12, %19, %20 sc1\n\t"
      "global_load_dwordx4 %5, %18, %20 offset:1024 sc1\n\tglobal_load_dwordx4 %13, %19, %20 offset:1024 sc1\n\t"
      "global_load_dwordx4 %6, %18, %20 offset:2048 sc1\n\tglobal_load_dwordx4 %14, %19, %20 offset:2048 sc1\n\t"
      "global_load_dwordx4 %7, %18, %20 offset:3072 sc1\n\tglobal_load_dwordx4 %15, %19, %20 offset:3072 sc1"
      : "=&v"(x[0][0]), "=&v"(x[0][1]), "=&v"(x[0][2]), "=&v"(x[0][3]), "=&v"(x[0][4]), "=&v"(x[0][5]), "=&v"(x[0][6]), "=&v"(x[0][7]),
        "=&v"(x[1][0]), "=&v"(x[1][1]), "=&v"(x[1][2]), "=&v"(x[1][3]), "=&v"(x[1][4]), "=&v"(x[1][5]), "=&v"(x[1][6]), "=&v"(x[1][7])
      : "v"(voff), "v"(voff + 128 * 1024), "v"(voff + 4096), "v"(voff + 128 * 1024 + 4096), "s"(sb) : "memory");
}
template <int N>
__device__ __forceinline__ void op_wait(v4u (&x)[2][8]) {
  asm volatile("s_waitcnt vmcnt(%16)"
      : "+v"(x[0][0]), "+v"(x[0][1]), "+v"(x[0][2]), "+v"(x[0][3]), "+v"(x[0][4]), "+v"(x[0][5]), "+v"(x[0][6]), "+v"(x[0][7]),
        "+v"(x[1][0]), "+v"(x[1][1]), "+v"(x[1][2]), "+v"(x[1][3]), "+v"(x[1][4]), "+v"(x[1][5]), "+v"(x[1][6]), "+v"(x[1][7])
      : "i"(N) : "memory");
}
template <int K0>
__device__ __forceinline__ void op_mma(v4f (&acc)[2][2], const v8bf (&W)[2][32], const v4u (&x)[2][8]) {
#pragma unroll
  for (int k = 0; k < 8; k += 2)
    asm volatile(
        "v_mfma_f32_16x16x32_bf16 %0, %4, %8, %0\n\tv_mfma_f32_16x16x32_bf16 %1, %6, %8, %1\n\t"
        "v_mfma_f32_16x16x32_bf16 %2, %4, %10, %2\n\tv_mfma_f32_16x16x32_bf16 %3, %6, %10, %3\n\t"
        "v_mfma_f32_16x16x32_bf16 %0, %5, %9, %0\n\tv_mfma_f32_16x16x32_bf16 %1, %7, %9, %1\n\t"
        "v_mfma_f32_16x16x32_bf16 %2, %5, %11, %2\n\tv_mfma_f32_16x16x32_bf16 %3, %7, %11, %3"
        : "+v"(acc[0][0]), "+v"(acc[0][1]), "+v"(acc[1][0]), "+v"(acc[1][1])
        : "a"(W[0][K0 + k]), "a"(W[0][K0 + k + 1]), "a"(W[1][K0 + k]), "a"(W[1][K0 + k + 1]),
          "v"(x[0][k]), "v"(x[0][k + 1]), "v"(x[1][k]), "v"(x[1][k + 1]));
}
__device__ __forceinline__ void op_role(const Params& p, char* lds, const int j, const int par, const int slotid) {
  const int tid = threadIdx.x, l = tid & 63, w = rfl(tid >> 6);
  u32* ctrl = (u32*)p.ws;
  const u32 ldsb = (u32)(size_t)(LAS char*)lds;
  const int g = l >> 4, c16 = l & 15;
  v8bf W[2][32];
  {
    const u32 reg = w * 32768;
    const int q = c16 >> 2, pp = l & 3;
#pragma unroll
    for (int r = 0; r < 2; ++r) {
#pragma unroll 1
      for (int n = 0; n < 32; ++n) glds16(p.wo + (size_t)(1024 * w + 512 * r + 16 * n + (l >> 2)) * 512 + 32 * j + (l & 3) * 8, ldsb + reg + n * 1024);
      wait_vm0();
#pragma unroll
      for (int kk = 0; kk < 16; ++kk)
#pragma unroll
        for (int T = 0; T < 2; ++T) {
          const u32 b = reg + (32 * kk + 8 * g + q) * 64 + T * 32 + pp * 8;
          const v2u x0 = tr_read(lds, b), x1 = tr_read(lds, b + 256);
          const v4u t = {x0[0], x0[1], x1[0], x1[1]};
          W[T][16 * r + kk] = __builtin_bit_cast(v8bf, t);
          asm volatile("" : "+a"(W[T][16 * r + kk]));
        }
      wait_lgkm0();
    }
  }
  bar();
  const u32 ooff = w * 32768 + l * 16;
  WSTAMP(1);
#pragma unroll 1
  for (int s = par; s < 64; s += 2) {
    {
      u32* word = ctrl + C_PROGO(8 * w + (l & 7), s & 3);
      u32 n = 0;
      while (!__all(ld_sc1(word) >= (u32)((s >> 2) + 1))) { if (spin_fail(n, ctrl)) break; __builtin_amdgcn_s_sleep(2); }
    }
    PSEG(0);
    v4f acc[2][2] = {{{0, 0, 0, 0}, {0, 0, 0, 0}}, {{0, 0, 0, 0}, {0, 0, 0, 0}}};
    const char* ob = p.ws + WS_O + (size_t)s * (2 * 128 * 1024);
    v4u xa[2][8], xb[2][8];
    op_issue(xa, ooff, ob); op_issue(xb, ooff, ob + 8192);
    asm volatile("s_nop 1" : "+v"(acc[0][0]), "+v"(acc[0][1]), "+v"(acc[1][0]), "+v"(acc[1][1]));
    op_wait<16>(xa); op_mma<0>(acc, W, xa); op_issue(xa, ooff, ob + 16384);
    op_wait<16>(xb); op_mma<8>(acc, W, xb); op_issue(xb, ooff, ob + 24576);
    op_wait<16>(xa); op_mma<16>(acc, W, xa);
    op_wait<0>(xb); op_mma<24>(acc, W, xb);
    asm volatile("s_nop 7" : "+v"(acc[0][0]), "+v"(acc[0][1]), "+v"(acc[1][0]), "+v"(acc[1][1]));
    PSEG(1);
    const u32 rb = ((s >> 1) & 1) * 16384;
#pragma unroll
    for (int tt = 0; tt < 2; ++tt)
#pragma unroll
      for (int T = 0; T < 2; ++T) *(LAS v4f*)(LAS char*)(lds + rb + (w * 4 + tt * 2 + T) * 1024 + l * 16) = acc[tt][T];
    bar();
    v4f sum = *(LAS v4f*)(LAS char*)(lds + rb + w * 1024 + l * 16);
#pragma unroll
    for (int k = 1; k < 4; ++k) sum += *(LAS v4f*)(LAS char*)(lds + rb + (k * 4 + w) * 1024 + l * 16);
    const v2u ov = {pack2(sum[0], sum[1]), pack2(sum[2], sum[3])};
    *(v2u*)(p.out + (size_t)(32 * s + 16 * (w >> 1) + c16) * 1024 + 512 + 32 * j + 16 * (w & 1) + 4 * g) = ov;
    PSEG(2);
    if ((s >> 1) == 0 || (s >> 1) == 8 || (s >> 1) == 16) WSTAMP(2 + (s >> 4));
  }
  WSTAMP(7);
}

__device__ __forceinline__ v2f cmul(v2f a, v2f b) { return (v2f){a[0] * b[0] - a[1] * b[1], a[0] * b[1] + a[1] * b[0]}; }
__device__ __forceinline__ v2f cmulc(v2f a, v2f b) { return (v2f){a[0] * b[0] + a[1] * b[1], a[1] * b[0] - a[0] * b[1]}; }
template <int INV> __device__ __forceinline__ v2f ctw(v2f a, v2f t) { return INV ? cmulc(a, t) : cmul(a, t); }
template <int INV> __device__ __forceinline__ v2f rot90(v2f a) { return INV ? (v2f){-a[1], a[0]} : (v2f){a[1], -a[0]}; }
template <int INV> __device__ __forceinline__ void fft4(v2f& x0, v2f& x1, v2f& x2, v2f& x3) {
  const v2f c0 = x0 + x2, c2 = x0 - x2, c1 = x1 + x3, c3 = rot90<INV>(x1 - x3);
  x0 = c0 + c1; x2 = c0 - c1; x1 = c2 + c3; x3 = c2 - c3;
}
template <int INV> __device__ __forceinline__ void fft8(v2f (&x)[8]) {
  constexpr float R = 0.70710678118654752f;
  v2f e0 = x[0] + x[4], o0 = x[0] - x[4], e1 = x[1] + x[5], o1 = x[1] - x[5], e2 = x[2] + x[6], o2 = x[2] - x[6], e3 = x[3] + x[7], o3 = x[3] - x[7];
  o1 = (o1 + rot90<INV>(o1)) * R; o2 = rot90<INV>(o2); o3 = (rot90<INV>(o3) - o3) * R;
  fft4<INV>(e0, e1, e2, e3); fft4<INV>(o0, o1, o2, o3);
  x[0] = e0; x[2] = e1; x[4] = e2; x[6] = e3; x[1] = o0; x[3] = o1; x[5] = o2; x[7] = o3;
}
constexpr u32 F_TW = 131072;
__device__ __forceinline__ v2f ldsv2(const char* lds, u32 off) { return *(LAS v2f*)(LAS char*)(lds + off); }
__device__ __forceinline__ void stsv2(char* lds, u32 off, v2f v) { *(LAS v2f*)(LAS char*)(lds + off) = v; }
__device__ __forceinline__ u32 fpos(u32 i) { return (i + 8u * (i >> 6)) * 8u; }
template <int INV> __device__ __forceinline__ void fft512_wave(v2f (&x)[8], char* lds, const u32 buf, const int l) {
#pragma unroll
  for (int m = 0; m < 8; ++m) x[m] = ldsv2(lds, buf + fpos(l + 64 * m));
  fft8<INV>(x);
#pragma unroll
  for (int q = 0; q < 8; ++q) stsv2(lds, buf + fpos(l + 64 * q), q ? ctw<INV>(x[q], ldsv2(lds, F_TW + (4 * l * q) * 8)) : x[q]);
  wait_lgkm0();
  const int q1 = l >> 3, j = l & 7;
#pragma unroll
  for (int m = 0; m < 8; ++m) x[m] = ldsv2(lds, buf + fpos(64 * q1 + j + 8 * m));
  fft8<INV>(x);
#pragma unroll
  for (int q = 0; q < 8; ++q) stsv2(lds, buf + fpos(64 * q1 + j + 8 * q), q ? ctw<INV>(x[q], ldsv2(lds, F_TW + (32 * j * q) * 8)) : x[q]);
  wait_lgkm0();
#pragma unroll
  for (int m = 0; m < 8; ++m) x[m] = ldsv2(lds, buf + fpos(8 * l + m));
  fft8<INV>(x);
}
__device__ __forceinline__ float bf2f(u32 h) { return u2f(h << 16); }
__device__ __forceinline__ void fft_barrier(u32* ctrl, const int k) {
  wait_vm0();
  bar();
  if (threadIdx.x == 0) __hip_atomic_fetch_add(ctrl + C_FFT(k), 1u, __ATOMIC_RELAXED, __HIP_MEMORY_SCOPE_AGENT);
  if (threadIdx.x < 64) wait_ge(ctrl, C_FFT(k), 32);
  bar();
}
template <int INV, int PASS> __device__ __forceinline__ void col_pass(char* lds, const int c, const int u) {
#pragma unroll 2
  for (int i = 0; i < 8; ++i) {
    int base, n;
    if (PASS == 1) { n = u + 32 * i; base = n; }
    else if (PASS == 2) { n = u; base = 256 * i + u; }
    else { n = u & 3; base = 32 * (8 * i + (u >> 2)) + n; }
    constexpr int stride = PASS == 1 ? 256 : (PASS == 2 ? 32 : 4), tws = PASS == 1 ? 1 : (PASS == 2 ? 8 : 64);
    v2f x[8];
#pragma unroll
    for (int m = 0; m < 8; ++m) {
      x[m] = ldsv2(lds, (base + stride * m) * 64 + c * 8);
      if (INV && m) x[m] = cmulc(x[m], ldsv2(lds, F_TW + (tws * n * m) * 8));
    }
    fft8<INV>(x);
#pragma unroll
    for (int q = 0; q < 8; ++q) {
      if (!INV && q) x[q] = cmul(x[q], ldsv2(lds, F_TW + (tws * n * q) * 8));
      stsv2(lds, (base + stride * q) * 64 + c * 8, x[q]);
    }
  }
}
__device__ __forceinline__ int row_freq(int r) { return (r >> 8) + 8 * ((r >> 5) & 7) + 64 * ((r >> 2) & 7) + 512 * (r & 3); }
__device__ __forceinline__ int freq_row(int k) { return 256 * (k & 7) + 32 * ((k >> 3) & 7) + 4 * ((k >> 6) & 7) + (k >> 9); }

__device__ __forceinline__ void fft_role(const Params& p, char* lds, const int fi) {
  const int tid = threadIdx.x, l = tid & 63, w = rfl(tid >> 6);
  u32* ctrl = (u32*)p.ws;
  for (int k = tid; k < 2048; k += 256) { float sn, cs; sincospif(-(float)k * (1.0f / 1024.0f), &sn, &cs); stsv2(lds, F_TW + k * 8, (v2f){cs, sn}); }
  bar();
  const __amdgpu_buffer_rsrc_t r1 = __builtin_amdgcn_make_buffer_rsrc(p.ws + WS_F1, 0, 8u * (u32)MiB, 0x00020000);
  const __amdgpu_buffer_rsrc_t r2 = __builtin_amdgcn_make_buffer_rsrc(p.ws + WS_F2, 0, 8u * (u32)MiB, 0x00020000);
  const u32 buf = w * 4608;
#pragma unroll 1
  for (int it = 0; it < 8; ++it) {
    const int pr = fi * 32 + it * 4 + w;
    const v4u ra = *(const v4u*)(p.ax + (size_t)(2 * pr) * 512 + 8 * l), rb = *(const v4u*)(p.ax + (size_t)(2 * pr + 1) * 512 + 8 * l);
#pragma unroll
    for (int i = 0; i < 4; ++i) {
      stsv2(lds, buf + fpos(8 * l + 2 * i), (v2f){bf2f(ra[i] & 0xffffu), bf2f(rb[i] & 0xffffu)});
      stsv2(lds, buf + fpos(8 * l + 2 * i + 1), (v2f){u2f(ra[i] & 0xffff0000u), u2f(rb[i] & 0xffff0000u)});
    }
    wait_lgkm0();
    v2f x[8];
    fft512_wave<0>(x, lds, buf, l);
#pragma unroll
    for (int m = 0; m < 8; ++m) stsv2(lds, buf + fpos((l >> 3) + 8 * (l & 7) + 64 * m), x[m]);
    wait_lgkm0();
#pragma unroll
    for (int i = 0; i < 4; ++i) {
      const int k = l + 64 * i;
      const v2f z = ldsv2(lds, buf + fpos(k)), y = ldsv2(lds, buf + fpos(k ? 512 - k : 256));
      v4f o = {0.5f * (z[0] + y[0]), 0.5f * (z[1] - y[1]), 0.5f * (z[1] + y[1]), 0.5f * (y[0] - z[0])};
      if (k == 0) o = (v4f){z[0], y[0], z[1], y[1]};
      __builtin_amdgcn_raw_buffer_store_b128(__builtin_bit_cast(v4u, o), r1, (((k >> 3) * 1024 + pr) * 8 + (k & 7)) * 16, 0, 16);
    }
    wait_lgkm0();
  }
  fft_barrier(ctrl, 0);
  {
#pragma unroll 1
    for (int b = 0; b < 4; ++b) {
      v4u t[8];
#pragma unroll
      for (int n = 0; n < 8; ++n) t[n] = ldb16(r1, (tid + 256 * (8 * b + n)) * 16, fi * 131072);
#pragma unroll
      for (int n = 0; n < 8; ++n) {
        const int e = tid + 256 * (8 * b + n);
        stsv2(lds, (2 * (e >> 3)) * 64 + (e & 7) * 8, (v2f){u2f(t[n][0]), u2f(t[n][1])});
        stsv2(lds, (2 * (e >> 3) + 1) * 64 + (e & 7) * 8, (v2f){u2f(t[n][2]), u2f(t[n][3])});
      }
    }
    const int c = tid & 7, u = tid >> 3;
    bar(); col_pass<0, 1>(lds, c, u);
    bar(); col_pass<0, 2>(lds, c, u);
    bar(); col_pass<0, 3>(lds, c, u);
    bar();
    constexpr float SC = 1.0f / (2048.0f * 512.0f);
    const bool packed = fi == 0 && c == 0;
    const int col = 8 * fi + c;
#pragma unroll 1
    for (int b = 0; b < 4; ++b) {
      float wr[4][4], wi[4][4];
#pragma unroll
      for (int i = 0; i < 4; ++i)
#pragma unroll
        for (int m = 0; m < 4; ++m) {
          const int k = row_freq(4 * (u + 32 * (4 * b + i))) + 512 * m;
          wr[i][m] = bf2f(p.awr[k * 257 + col]) * SC; wi[i][m] = bf2f(p.awi[k * 257 + col]) * SC;
        }
#pragma unroll
      for (int i = 0; i < 4; ++i) {
        const int r0 = 4 * (u + 32 * (4 * b + i));
        v2f x0 = ldsv2(lds, r0 * 64 + c * 8), x1 = ldsv2(lds, (r0 + 1) * 64 + c * 8), x2 = ldsv2(lds, (r0 + 2) * 64 + c * 8), x3 = ldsv2(lds, (r0 + 3) * 64 + c * 8);
        fft4<0>(x0, x1, x2, x3);
        if (!packed) {
          x0 = cmul(x0, (v2f){wr[i][0], wi[i][0]}); x1 = cmul(x1, (v2f){wr[i][1], wi[i][1]});
          x2 = cmul(x2, (v2f){wr[i][2], wi[i][2]}); x3 = cmul(x3, (v2f){wr[i][3], wi[i][3]});
          fft4<1>(x0, x1, x2, x3);
        }
        stsv2(lds, r0 * 64 + c * 8, x0); stsv2(lds, (r0 + 1) * 64 + c * 8, x1); stsv2(lds, (r0 + 2) * 64 + c * 8, x2); stsv2(lds, (r0 + 3) * 64 + c * 8, x3);
      }
    }
    if (fi == 0) {
      bar();
      for (int k = tid; k <= 1024; k += 256) {
        const int kn = (2048 - k) & 2047;
        const v2f z = ldsv2(lds, freq_row(k) * 64), y = ldsv2(lds, freq_row(kn) * 64);
        const v2f A = {0.5f * (z[0] + y[0]), 0.5f * (z[1] - y[1])}, B = {0.5f * (z[1] + y[1]), 0.5f * (y[0] - z[0])};
        const v2f An = {A[0], -A[1]}, Bn = {B[0], -B[1]};
        const v2f a1 = cmul(A, (v2f){bf2f(p.awr[k * 257]) * SC, bf2f(p.awi[k * 257]) * SC}), a2 = cmul(An, (v2f){bf2f(p.awr[kn * 257]) * SC, bf2f(p.awi[kn * 257]) * SC});
        const v2f b1 = cmul(B, (v2f){bf2f(p.awr[k * 257 + 256]) * SC, bf2f(p.awi[k * 257 + 256]) * SC}), b2 = cmul(Bn, (v2f){bf2f(p.awr[kn * 257 + 256]) * SC, bf2f(p.awi[kn * 257 + 256]) * SC});
        const v2f Ah = {0.5f * (a1[0] + a2[0]), 0.5f * (a1[1] - a2[1])}, Bh = {0.5f * (b1[0] + b2[0]), 0.5f * (b1[1] - b2[1])};
        stsv2(lds, freq_row(k) * 64, (v2f){Ah[0] - Bh[1], Ah[1] + Bh[0]});
        if (kn != k) stsv2(lds, freq_row(kn) * 64, (v2f){Ah[0] + Bh[1], Bh[0] - Ah[1]});
      }
      bar();
      for (int b = tid; b < 512; b += 256) {
        v2f x0 = ldsv2(lds, (4 * b) * 64), x1 = ldsv2(lds, (4 * b + 1) * 64), x2 = ldsv2(lds, (4 * b + 2) * 64), x3 = ldsv2(lds, (4 * b + 3) * 64);
        fft4<1>(x0, x1, x2, x3);
        stsv2(lds, (4 * b) * 64, x0); stsv2(lds, (4 * b + 1) * 64, x1); stsv2(lds, (4 * b + 2) * 64, x2); stsv2(lds, (4 * b + 3) * 64, x3);
      }
    }
    bar(); col_pass<1, 3>(lds, c, u);
    bar(); col_pass<1, 2>(lds, c, u);
    bar(); col_pass<1, 1>(lds, c, u);
    bar();
#pragma unroll 4
    for (int n = 0; n < 32; ++n) {
      const int e = tid + 256 * n, pr = e >> 3;
      const v2f a = ldsv2(lds, (2 * pr) * 64 + (e & 7) * 8), b = ldsv2(lds, (2 * pr + 1) * 64 + (e & 7) * 8);
      __builtin_amdgcn_raw_buffer_store_b128(__builtin_bit_cast(v4u, (v4f){a[0], a[1], b[0], b[1]}), r2, (pr * 256 + 8 * fi + (e & 7)) * 16, 0, 16);
    }
  }
  fft_barrier(ctrl, 1);
#pragma unroll 1
  for (int it = 0; it < 8; ++it) {
    const int pr = fi * 32 + it * 4 + w;
    v4u t[4];
#pragma unroll
    for (int i = 0; i < 4; ++i) t[i] = ldb16(r2, (l + 64 * i) * 16, pr * 4096);
#pragma unroll
    for (int i = 0; i < 4; ++i) {
      const int k = l + 64 * i;
      const float ar = u2f(t[i][0]), ai = u2f(t[i][1]), br = u2f(t[i][2]), bi = u2f(t[i][3]);
      if (k) { stsv2(lds, buf + fpos(k), (v2f){ar - bi, ai + br}); stsv2(lds, buf + fpos(512 - k), (v2f){ar + bi, br - ai}); }
      else { stsv2(lds, buf + fpos(0), (v2f){ar, br}); stsv2(lds, buf + fpos(256), (v2f){ai, bi}); }
    }
    wait_lgkm0();
    v2f x[8];
    fft512_wave<1>(x, lds, buf, l);
#pragma unroll
    for (int m = 0; m < 8; ++m) stsv2(lds, buf + fpos((l >> 3) + 8 * (l & 7) + 64 * m), x[m]);
    wait_lgkm0();
    v4u oa, ob;
#pragma unroll
    for (int i = 0; i < 4; ++i) {
      const v2f a = ldsv2(lds, buf + fpos(8 * l + 2 * i)), b = ldsv2(lds, buf + fpos(8 * l + 2 * i + 1));
      oa[i] = pack2(a[0], b[0]); ob[i] = pack2(a[1], b[1]);
    }
    *(v4u*)(p.out + (size_t)(2 * pr) * 1024 + 8 * l) = oa;
    *(v4u*)(p.out + (size_t)(2 * pr + 1) * 1024 + 8 * l) = ob;
    wait_lgkm0();
  }
}

constexpr int LDS_BYTES = 3 * 49152;

__global__ void __launch_bounds__(256, 1) fused_kernel(Params p) {
  extern __shared__ __attribute__((aligned(16))) char lds[];
  u32* ctrl = (u32*)p.ws;
  int slot;
  {
    LAS u32* sh = (LAS u32*)(LAS char*)lds;
    if (threadIdx.x == 0) {
      const u32 x = xcc_id();
      const u32 i = __hip_atomic_fetch_add(ctrl + C_CLAIM(x), 1u, __ATOMIC_RELAXED, __HIP_MEMORY_SCOPE_AGENT);
      u32 sl = x * 32 + i;
      if (i >= 32) {
        u32 k = __hip_atomic_fetch_add(ctrl + C_OVER, 1u, __ATOMIC_RELAXED, __HIP_MEMORY_SCOPE_AGENT);
        u32 n = 0;
        while (true) {
          u32 sum = 0;
#pragma unroll
          for (int j = 0; j < 8; ++j) sum += __hip_atomic_load(ctrl + C_CLAIM(j), __ATOMIC_RELAXED, __HIP_MEMORY_SCOPE_AGENT);
          if (sum >= 256u) break;
          if (++n > SPIN_CAP) { st_sc1(ctrl + C_ABORT, 1u); break; }
        }
        sl = 255;
        bool found = false;
#pragma unroll 1
        for (int j = 0; j < 8; ++j) {
          const u32 cj = __hip_atomic_load(ctrl + C_CLAIM(j), __ATOMIC_RELAXED, __HIP_MEMORY_SCOPE_AGENT);
          const u32 open = cj < 32 ? 32 - cj : 0;
          if (!found && k < open) { sl = j * 32 + cj + k; found = true; }
          if (!found) k -= open;
        }
      }
      sh[0] = sl;
    }
    __syncthreads();
    slot = rfl(sh[0]);
    __syncthreads();
  }
#define ROLEMASK 31
  if (slot < 128) { if (ROLEMASK & 16) h_role(p, lds, slot); }
  else {
    const int x = (slot - 128) >> 5, i = slot & 31, h = 8 * x + (i & 7), kind = i >> 3;
    if (!((ROLEMASK >> kind) & 1)) return;
    if (kind == 0) proj_role<2>(p, lds, h, slot);
    else if (kind == 1) { proj_role<1>(p, lds, h, slot); fft_role(p, lds, h);
    }
    else if (kind == 2) attn_role(p, lds, h, slot);
    else op_role(p, lds, 4 * x + (i & 3), (i >> 2) & 1, slot);
  }
}

extern "C" void kernel_launch(void* const* d_in, const int* in_sizes, int n_in, void* d_out, int out_size, void* d_ws, size_t ws_size, hipStream_t stream) {
  Params p;
  p.ax = (const u16*)d_in[0]; p.awr = (const u16*)d_in[1]; p.awi = (const u16*)d_in[2];
  p.bx = (const u16*)d_in[3]; p.mem0 = (const u16*)d_in[4]; p.wq = (const u16*)d_in[5]; p.wk = (const u16*)d_in[6]; p.wv = (const u16*)d_in[7]; p.wo = (const u16*)d_in[8];
  p.out = (u16*)d_out; p.ws = (char*)d_ws;
  hipFuncSetAttribute((const void*)fused_kernel, hipFuncAttributeMaxDynamicSharedMemorySize, LDS_BYTES);
  hipMemsetAsync(d_ws, 0, WS_CTRL_BYTES, stream);
  hipLaunchKernelGGL(fused_kernel, dim3(256), dim3(256), LDS_BYTES, stream, p);
}
```
